```python
import torch

def _softmax(x, axis=-1):
    return torch.softmax(x, dim=axis)
def _gelu(x):
    return torch.nn.functional.gelu(x, approximate="tanh")
def _silu(x):
    return torch.nn.functional.silu(x)
def _sigmoid(x):
    return torch.sigmoid(x)
def _layernorm(x, axis=-1, eps=1e-5):
    m = x.mean(dim=axis, keepdim=True)
    v = x.var(dim=axis, keepdim=True, unbiased=False)
    return (x - m) / torch.sqrt(v + eps)
def _scatter_add(tgt, idx, src, axis=0):
    return tgt.clone().index_add_(axis, idx, src)
def _segment_sum(src, seg, n):
    out = torch.zeros((n,) + src.shape[1:], dtype=src.dtype)
    return out.index_add_(0, seg, src)
def _pad(x, pw):
    flat = tuple(v for pair in reversed(pw) for v in pair)
    return torch.nn.functional.pad(x, flat)

kernel_name = 'sequential_preLN__mha_block_cached__glu_gated_bilinear__decode'

def setup_inputs():

    _w = (lambda a: (lambda t: t.to(torch.bfloat16) if t.is_floating_point() else t)(torch.as_tensor(a)))
    def _A_setup():
        B, Sq, D = 2, 1, 640
        H, Sk, Dh = 32, 4096, 128
        return {
            "x": _w((lambda *s: torch.randn(*s, dtype=torch.bfloat16) * 0.05)(B, Sq, D)),
            "k_cache": _w((lambda *s: torch.randn(*s, dtype=torch.bfloat16) * 1.6)(B, H, Sk, Dh)),
            "v_cache": _w((lambda *s: torch.randn(*s, dtype=torch.bfloat16) * 0.8)(B, H, Sk, Dh)),
            "wq": _w((lambda *s: torch.randn(*s, dtype=torch.bfloat16) * 1.6)(D, H * Dh) * (D ** -0.5)),
            "wo": _w((lambda *s: torch.randn(*s, dtype=torch.bfloat16) * 0.8)(H * Dh, D) * ((H * Dh) ** -0.5)),
        }
    _A_in = _A_setup()

    def _B_setup():
        B, S, D, F = 2, 1, 640, 1728
        return {
            "x": _w((lambda *s: torch.randn(*s, dtype=torch.bfloat16) * 0.05)(B, S, D)),
            "w_gate": _w((lambda *s: torch.randn(*s, dtype=torch.bfloat16) * 0.4)(D, F) * (D ** -0.5)),
            "w_up": _w((lambda *s: torch.randn(*s, dtype=torch.bfloat16) * 0.4)(D, F) * (D ** -0.5)),
            "w_down": _w((lambda *s: torch.randn(*s, dtype=torch.bfloat16) * 0.4)(F, D) * (F ** -0.5)),
        }
    _B_in = _B_setup()

    return {
        "A_x": _A_in["x"],
        "A_k_cache": _A_in["k_cache"],
        "A_v_cache": _A_in["v_cache"],
        "A_wq": _A_in["wq"],
        "A_wo": _A_in["wo"],
        "B_x": _B_in["x"],
        "B_w_gate": _B_in["w_gate"],
        "B_w_up": _B_in["w_up"],
        "B_w_down": _B_in["w_down"],
    }

def reference(A_x, A_k_cache, A_v_cache, A_wq, A_wo, B_x, B_w_gate, B_w_up, B_w_down):
    def _A_ref(x, k_cache, v_cache, wq, wo):
        B, Sq, D = x.shape
        H, Dh = 32, 128
        q = (lambda x, *d: x.permute(*d))((x @ wq).reshape(B, Sq, H, Dh), 0, 2, 1, 3)
        scores = torch.einsum("bhqd,bhkd->bhqk", q, k_cache) * (Dh ** -0.5)
        p = _softmax(scores, axis=-1)
        o = torch.einsum("bhqk,bhkd->bhqd", p, v_cache)
        return (lambda x, *d: x.permute(*d))(o, 0, 2, 1, 3).reshape(B, Sq, H * Dh) @ wo

    def _B_ref(x, w_gate, w_up, w_down):
        g = torch.einsum("bsd,df->bsf", x, w_gate)
        u = torch.einsum("bsd,df->bsf", x, w_up)
        h = g * u
        return torch.einsum("bsf,fd->bsd", h, w_down)

    x = A_x
    h = x + _A_ref(_layernorm(x, axis=-1), A_k_cache, A_v_cache, A_wq, A_wo)
    h2 = h + _B_ref(_layernorm(h, axis=-1), B_w_gate, B_w_up, B_w_down)
    return h2
```

```cpp
#include <hip/hip_runtime.h>
#include <hip/hip_bf16.h>
#include <cstdint>

#define GAS __attribute__((address_space(1)))
typedef unsigned v4u __attribute__((ext_vector_type(4)));
typedef float f32x4 __attribute__((ext_vector_type(4)));
typedef __bf16 bf16x2 __attribute__((ext_vector_type(2)));
typedef unsigned short u16;

constexpr int B = 2, D = 640, H = 32, SK = 4096, DH = 128, F = 1728;
constexpr int NSPLIT = 4, KEYS_WG = SK / NSPLIT;
constexpr float EPS = 1e-5f;
constexpr float C2 = 0.088388347648318440f * 1.4426950408889634f;

__device__ __forceinline__ float dot2(unsigned k, unsigned q, float acc) {
  return __builtin_amdgcn_fdot2_f32_bf16(__builtin_bit_cast(bf16x2, k), __builtin_bit_cast(bf16x2, q), acc, false);
}
template <int CTRL> __device__ __forceinline__ float dpp(float x) {
  return __builtin_bit_cast(float, __builtin_amdgcn_mov_dpp(__builtin_bit_cast(int, x), CTRL, 0xf, 0xf, true));
}
__device__ __forceinline__ float bflo(unsigned u) { return __builtin_bit_cast(float, u << 16); }
__device__ __forceinline__ float bfhi(unsigned u) { return __builtin_bit_cast(float, u & 0xffff0000u); }
__device__ __forceinline__ float bf2f(u16 u) { return __builtin_bit_cast(float, (unsigned)u << 16); }
__device__ __forceinline__ unsigned cvtpk(float lo, float hi) {
  unsigned r; asm volatile("v_cvt_pk_bf16_f32 %0, %1, %2" : "=v"(r) : "v"(lo), "v"(hi)); return r;
}
constexpr int XOR1 = 0xB1, XOR2 = 0x4E, XOR7 = 0x141, XOR8 = 0x128, ROR4 = 0x124;
__device__ __forceinline__ float xrow16_max(float x) {
  auto s = __builtin_amdgcn_permlane16_swap(__float_as_uint(x), __float_as_uint(x), false, false);
  x = fmaxf(__uint_as_float(s[0]), __uint_as_float(s[1]));
  auto t = __builtin_amdgcn_permlane32_swap(__float_as_uint(x), __float_as_uint(x), false, false);
  return fmaxf(__uint_as_float(t[0]), __uint_as_float(t[1]));
}
__device__ __forceinline__ float xrow16_sum(float x) {
  auto s = __builtin_amdgcn_permlane16_swap(__float_as_uint(x), __float_as_uint(x), false, false);
  x = __uint_as_float(s[0]) + __uint_as_float(s[1]);
  auto t = __builtin_amdgcn_permlane32_swap(__float_as_uint(x), __float_as_uint(x), false, false);
  return __uint_as_float(t[0]) + __uint_as_float(t[1]);
}
__device__ __forceinline__ float row16_sum(float x) {
  x += dpp<XOR1>(x); x += dpp<XOR2>(x); x += dpp<XOR7>(x); x += dpp<XOR8>(x); return x;
}
__device__ __forceinline__ float row16_max(float x) {
  x = fmaxf(x, dpp<XOR1>(x)); x = fmaxf(x, dpp<XOR2>(x)); x = fmaxf(x, dpp<XOR7>(x)); x = fmaxf(x, dpp<XOR8>(x)); return x;
}
__device__ __forceinline__ float wave_sum(float x) { return xrow16_sum(row16_sum(x)); }
__device__ __forceinline__ float wave_max(float x) { return xrow16_max(row16_max(x)); }
__device__ __forceinline__ v4u ldnt(const GAS v4u* p) { return __builtin_nontemporal_load(p); }

template <int I> __device__ __forceinline__ void fma8_bc(float (&acc)[8], float p, v4u v) {
  const float pb = dpp<0x150 + I>(p);
#pragma unroll
  for (int j = 0; j < 4; ++j) { acc[2 * j] = fmaf(pb, bflo(v[j]), acc[2 * j]); acc[2 * j + 1] = fmaf(pb, bfhi(v[j]), acc[2 * j + 1]); }
}
template <int I, int N> struct Unroll {
  template <typename Fn> __device__ static __forceinline__ void run(Fn&& f) { f(std::integral_constant<int, I>{}); Unroll<I + 1, N>::run(f); }
};
template <int N> struct Unroll<N, N> { template <typename Fn> __device__ static __forceinline__ void run(Fn&&) {} };

__device__ __forceinline__ float tr16(float (&x)[16], int c) {
#define TR_STEP(HALF, CTRL, BIT) _Pragma("unroll") for (int h = 0; h < (HALF); ++h) { \
    const float lo_ = x[h], hi_ = x[h + (HALF)]; const float t1_ = lo_ + dpp<CTRL>(lo_), t2_ = hi_ + dpp<CTRL>(hi_); \
    x[h] = (c & (BIT)) ? t2_ : t1_; }
  TR_STEP(8, XOR8, 8) TR_STEP(4, XOR7, 4) TR_STEP(2, XOR2, 2) TR_STEP(1, XOR1, 1)
#undef TR_STEP
  return x[0];
}

__global__ void __launch_bounds__(512, 2) k_attn(const u16* __restrict__ x, const u16* __restrict__ kc, const u16* __restrict__ vc,
                                                 const u16* __restrict__ wq, float* __restrict__ part) {
  __shared__ __attribute__((aligned(16))) float lds[640 + 8 * 132 + 64 + 16];
  float* xn = lds;
  float* red = lds + 640;
  unsigned* qs = (unsigned*)(lds + 640 + 8 * 132);
  const int tid = threadIdx.x, lane = tid & 63, w = __builtin_amdgcn_readfirstlane(tid >> 6), r = lane >> 4, c = lane & 15;
  const int bid = blockIdx.x, xcd = bid & 7, j = bid >> 3, h = xcd * 4 + (j >> 3), b = (j >> 2) & 1, s = j & 3;
  const GAS v4u* wqp = (const GAS v4u*)(wq + (size_t)(80 * w + r) * (H * DH) + h * DH + 8 * c);
  v4u wr[20];
#pragma unroll
  for (int i = 0; i < 20; ++i) wr[i] = wqp[(size_t)i * 4 * (H * DH) / 8];
  unsigned xr[5];
  {
    const GAS unsigned* xp = (const GAS unsigned*)(x + b * D);
#pragma unroll
    for (int i = 0; i < 5; ++i) xr[i] = xp[lane + 64 * i];
  }
  const size_t kvoff = ((size_t)(b * H + h) * SK + s * KEYS_WG + w * 128) * DH;
  const GAS v4u* kp = (const GAS v4u*)(kc + kvoff) + lane;
  const GAS v4u* vp = (const GAS v4u*)(vc + kvoff) + lane;
  v4u kv[32];
#pragma unroll
  for (int i = 0; i < 32; ++i) kv[i] = ldnt(kp + i * 64);
  __builtin_amdgcn_sched_barrier(0);
  {
    float sum = 0.f;
#pragma unroll
    for (int i = 0; i < 5; ++i) sum += bflo(xr[i]) + bfhi(xr[i]);
    const float mean = wave_sum(sum) * (1.f / D);
    float sq = 0.f;
#pragma unroll
    for (int i = 0; i < 5; ++i) { const float a = bflo(xr[i]) - mean, bb = bfhi(xr[i]) - mean; sq += a * a + bb * bb; }
    const float rstd = 1.0f / sqrtf(wave_sum(sq) * (1.f / D) + EPS);
    if (w == 0) {
#pragma unroll
      for (int i = 0; i < 5; ++i) { xn[2 * (lane + 64 * i)] = (bflo(xr[i]) - mean) * rstd; xn[2 * (lane + 64 * i) + 1] = (bfhi(xr[i]) - mean) * rstd; }
    }
  }
  __syncthreads();
  {
    float acc[8] = {0, 0, 0, 0, 0, 0, 0, 0};
#pragma unroll
    for (int i = 0; i < 20; ++i) {
      const float xv = xn[80 * w + 4 * i + r];
#pragma unroll
      for (int jj = 0; jj < 4; ++jj) { acc[2 * jj] = fmaf(xv, bflo(wr[i][jj]), acc[2 * jj]); acc[2 * jj + 1] = fmaf(xv, bfhi(wr[i][jj]), acc[2 * jj + 1]); }
    }
#pragma unroll
    for (int jj = 0; jj < 8; ++jj) acc[jj] = xrow16_sum(acc[jj]);
    if (r == 0) {
      *(f32x4*)(red + w * 132 + 8 * c) = f32x4{acc[0], acc[1], acc[2], acc[3]};
      *(f32x4*)(red + w * 132 + 8 * c + 4) = f32x4{acc[4], acc[5], acc[6], acc[7]};
    }
  }
  __syncthreads();
  if (tid < 64) {
    float a0 = 0.f, a1 = 0.f;
#pragma unroll
    for (int ww = 0; ww < 8; ++ww) { a0 += red[ww * 132 + 2 * tid]; a1 += red[ww * 132 + 2 * tid + 1]; }
    qs[tid] = cvtpk(a0, a1);
  }
  __syncthreads();
  const v4u q = *(const v4u*)(qs + 4 * c);
  float sc[2];
#pragma unroll
  for (int g = 0; g < 2; ++g) {
    float xs[16];
#pragma unroll
    for (int i = 0; i < 16; ++i) {
      const v4u kk = kv[g * 16 + i];
      float d = dot2(kk[0], q[0], 0.f); d = dot2(kk[1], q[1], d); d = dot2(kk[2], q[2], d); d = dot2(kk[3], q[3], d);
      xs[i] = d;
      kv[g * 16 + i] = ldnt(vp + (g * 16 + i) * 64);
      __builtin_amdgcn_sched_barrier(0);
    }
    sc[g] = tr16(xs, c) * C2;
  }
  const float m = wave_max(fmaxf(sc[0], sc[1]));
  const float p0 = __builtin_amdgcn_exp2f(sc[0] - m), p1 = __builtin_amdgcn_exp2f(sc[1] - m);
  const float l = wave_sum(p0 + p1);
  float acc[8] = {0, 0, 0, 0, 0, 0, 0, 0};
  Unroll<0, 16>::run([&](auto I) { fma8_bc<I.value>(acc, p0, kv[I.value]); });
  Unroll<0, 16>::run([&](auto I) { fma8_bc<I.value>(acc, p1, kv[16 + I.value]); });
#pragma unroll
  for (int jj = 0; jj < 8; ++jj) acc[jj] = xrow16_sum(acc[jj]);
  if (r == 0) {
    *(f32x4*)(red + w * 132 + 8 * c) = f32x4{acc[0], acc[1], acc[2], acc[3]};
    *(f32x4*)(red + w * 132 + 8 * c + 4) = f32x4{acc[4], acc[5], acc[6], acc[7]};
    if (c == 0) { red[w * 132 + 128] = m; red[w * 132 + 129] = l; }
  }
  __syncthreads();
  if (tid < 128) {
    float M = red[128];
#pragma unroll
    for (int ww = 1; ww < 8; ++ww) M = fmaxf(M, red[ww * 132 + 128]);
    float L = 0.f, o = 0.f;
#pragma unroll
    for (int ww = 0; ww < 8; ++ww) { const float wt = __builtin_amdgcn_exp2f(red[ww * 132 + 128] - M); L = fmaf(wt, red[ww * 132 + 129], L); o = fmaf(wt, red[ww * 132 + tid], o); }
    float* pp = part + (size_t)((b * H + h) * NSPLIT + s) * 132;
    pp[tid] = o;
    if (tid == 0) { pp[128] = M; pp[129] = L; }
  }
}

__global__ void __launch_bounds__(256) k_oproj(const float* __restrict__ part, const u16* __restrict__ wo, float* __restrict__ hp) {
  __shared__ float o[2][512];
  __shared__ float red[64][2][32];
  const int tid = threadIdx.x, ng = blockIdx.x, kg = blockIdx.y;
  v4u wr[8];
  const GAS v4u* wp = (const GAS v4u*)(wo + (size_t)(kg * 512 + (tid >> 2)) * D + ng * 32 + 8 * (tid & 3));
#pragma unroll
  for (int i = 0; i < 8; ++i) wr[i] = wp[(size_t)i * 64 * D / 8];
  for (int e = tid; e < 1024; e += 256) {
    const int b = e >> 9, k = e & 511, h = kg * 4 + (k >> 7), d = k & 127;
    const float* pp = part + (size_t)((b * H + h) * NSPLIT) * 132;
    float M = pp[128];
#pragma unroll
    for (int s = 1; s < NSPLIT; ++s) M = fmaxf(M, pp[s * 132 + 128]);
    float L = 0.f, a = 0.f;
#pragma unroll
    for (int s = 0; s < NSPLIT; ++s) { const float wt = __builtin_amdgcn_exp2f(pp[s * 132 + 128] - M); L = fmaf(wt, pp[s * 132 + 129], L); a = fmaf(wt, pp[s * 132 + d], a); }
    o[b][k] = a / L;
  }
  __syncthreads();
  float acc[2][8] = {};
#pragma unroll
  for (int i = 0; i < 8; ++i) {
    const int k = (tid >> 2) + 64 * i;
    const float o0 = o[0][k], o1 = o[1][k];
#pragma unroll
    for (int j = 0; j < 4; ++j) {
      acc[0][2 * j] = fmaf(o0, bflo(wr[i][j]), acc[0][2 * j]); acc[0][2 * j + 1] = fmaf(o0, bfhi(wr[i][j]), acc[0][2 * j + 1]);
      acc[1][2 * j] = fmaf(o1, bflo(wr[i][j]), acc[1][2 * j]); acc[1][2 * j + 1] = fmaf(o1, bfhi(wr[i][j]), acc[1][2 * j + 1]);
    }
  }
#pragma unroll
  for (int b = 0; b < 2; ++b)
#pragma unroll
    for (int j = 0; j < 8; ++j) red[tid >> 2][b][8 * (tid & 3) + j] = acc[b][j];
  __syncthreads();
  if (tid < 64) {
    const int b = tid >> 5, n = tid & 31;
    float sum = 0.f;
    for (int i = 0; i < 64; ++i) sum += red[i][b][n];
    hp[(kg * 2 + b) * D + ng * 32 + n] = sum;
  }
}

__global__ void __launch_bounds__(256) k_gu(const u16* __restrict__ x, const float* __restrict__ hp, const u16* __restrict__ wg, const u16* __restrict__ wu,
                                            float* __restrict__ hh, float* __restrict__ hout) {
  __shared__ float hn[2][640];
  __shared__ float red[64][4][32];
  __shared__ float st[8];
  const int tid = threadIdx.x, fg = blockIdx.x, lane = tid & 63, w = tid >> 6;
  v4u gr[10], ur[10];
  const size_t woff = (size_t)(tid >> 2) * F + fg * 32 + 8 * (tid & 3);
  const GAS v4u* gp = (const GAS v4u*)(wg + woff);
  const GAS v4u* up = (const GAS v4u*)(wu + woff);
#pragma unroll
  for (int i = 0; i < 10; ++i) { gr[i] = gp[(size_t)i * 64 * F / 8]; ur[i] = up[(size_t)i * 64 * F / 8]; }
  for (int e = tid; e < 1280; e += 256) {
    const int b = e / 640, n = e % 640;
    float v = bf2f(x[e]);
    float a = 0.f;
#pragma unroll
    for (int kg = 0; kg < 8; ++kg) a += hp[(kg * 2 + b) * D + n];
    v += a;
    hn[b][n] = v;
    if (fg == 0) hout[e] = v;
  }
  __syncthreads();
  if (w < 2) {
    float sum = 0.f;
    for (int i = lane; i < 640; i += 64) sum += hn[w][i];
    const float mean = wave_sum(sum) * (1.f / D);
    float sq = 0.f;
    for (int i = lane; i < 640; i += 64) { const float a = hn[w][i] - mean; sq += a * a; }
    const float rstd = 1.0f / sqrtf(wave_sum(sq) * (1.f / D) + EPS);
    if (lane == 0) { st[2 * w] = mean; st[2 * w + 1] = rstd; }
  }
  __syncthreads();
  float acc[4][8] = {};
#pragma unroll
  for (int i = 0; i < 10; ++i) {
    const int k = (tid >> 2) + 64 * i;
    const float h0 = (hn[0][k] - st[0]) * st[1], h1 = (hn[1][k] - st[2]) * st[3];
#pragma unroll
    for (int j = 0; j < 4; ++j) {
      acc[0][2 * j] = fmaf(h0, bflo(gr[i][j]), acc[0][2 * j]); acc[0][2 * j + 1] = fmaf(h0, bfhi(gr[i][j]), acc[0][2 * j + 1]);
      acc[1][2 * j] = fmaf(h1, bflo(gr[i][j]), acc[1][2 * j]); acc[1][2 * j + 1] = fmaf(h1, bfhi(gr[i][j]), acc[1][2 * j + 1]);
      acc[2][2 * j] = fmaf(h0, bflo(ur[i][j]), acc[2][2 * j]); acc[2][2 * j + 1] = fmaf(h0, bfhi(ur[i][j]), acc[2][2 * j + 1]);
      acc[3][2 * j] = fmaf(h1, bflo(ur[i][j]), acc[3][2 * j]); acc[3][2 * j + 1] = fmaf(h1, bfhi(ur[i][j]), acc[3][2 * j + 1]);
    }
  }
#pragma unroll
  for (int a = 0; a < 4; ++a)
#pragma unroll
    for (int j = 0; j < 8; ++j) red[tid >> 2][a][8 * (tid & 3) + j] = acc[a][j];
  __syncthreads();
  if (tid < 64) {
    const int b = tid >> 5, n = tid & 31;
    float g = 0.f, u = 0.f;
    for (int i = 0; i < 64; ++i) { g += red[i][b][n]; u += red[i][2 + b][n]; }
    hh[b * F + fg * 32 + n] = g * u;
  }
}

__global__ void __launch_bounds__(256) k_down(const float* __restrict__ hh, const float* __restrict__ hres, const u16* __restrict__ wd, u16* __restrict__ out) {
  __shared__ float red[128][2][16];
  const int tid = threadIdx.x, ng = blockIdx.x;
  v4u wr[14];
  const GAS v4u* wp = (const GAS v4u*)(wd + (size_t)(tid >> 1) * D + ng * 16 + 8 * (tid & 1));
#pragma unroll
  for (int i = 0; i < 14; ++i) { const int k = (tid >> 1) + 128 * i; wr[i] = (k < F) ? wp[(size_t)i * 128 * D / 8] : v4u{0, 0, 0, 0}; }
  float acc[2][8] = {};
#pragma unroll
  for (int i = 0; i < 14; ++i) {
    const int k = (tid >> 1) + 128 * i;
    const float h0 = (k < F) ? hh[k] : 0.f, h1 = (k < F) ? hh[F + k] : 0.f;
#pragma unroll
    for (int j = 0; j < 4; ++j) {
      acc[0][2 * j] = fmaf(h0, bflo(wr[i][j]), acc[0][2 * j]); acc[0][2 * j + 1] = fmaf(h0, bfhi(wr[i][j]), acc[0][2 * j + 1]);
      acc[1][2 * j] = fmaf(h1, bflo(wr[i][j]), acc[1][2 * j]); acc[1][2 * j + 1] = fmaf(h1, bfhi(wr[i][j]), acc[1][2 * j + 1]);
    }
  }
#pragma unroll
  for (int b = 0; b < 2; ++b)
#pragma unroll
    for (int j = 0; j < 8; ++j) red[tid >> 1][b][8 * (tid & 1) + j] = acc[b][j];
  __syncthreads();
  if (tid < 32) {
    const int b = tid >> 4, n = tid & 15;
    float sum = 0.f;
    for (int i = 0; i < 128; ++i) sum += red[i][b][n];
    const float v = hres[b * D + ng * 16 + n] + sum;
    out[b * D + ng * 16 + n] = (u16)(cvtpk(v, v) & 0xffffu);
  }
}

extern "C" void kernel_launch(void* const* d_in, const int* in_sizes, int n_in, void* d_out, int out_size, void* d_ws, size_t ws_size,
                              hipStream_t stream) {
  const u16* x = (const u16*)d_in[0];
  const u16* kc = (const u16*)d_in[1];
  const u16* vc = (const u16*)d_in[2];
  const u16* wq = (const u16*)d_in[3];
  const u16* wo = (const u16*)d_in[4];
  const u16* wg = (const u16*)d_in[6];
  const u16* wu = (const u16*)d_in[7];
  const u16* wd = (const u16*)d_in[8];
  float* ws = (float*)d_ws;
  float* part = ws;
  float* hp = ws + 65536;
  float* hh = ws + 65536 + 16384;
  float* hres = ws + 65536 + 16384 + 4096;
  k_attn<<<256, 512, 0, stream>>>(x, kc, vc, wq, part);
  k_oproj<<<dim3(20, 8), 256, 0, stream>>>(part, wo, hp);
  k_gu<<<54, 256, 0, stream>>>(x, hp, wg, wu, hh, hres);
  k_down<<<40, 256, 0, stream>>>(hh, hres, wd, (u16*)d_out);
}
```

```cpp
#include <hip/hip_runtime.h>
#include <hip/hip_bf16.h>
#include <cstdint>
#include <type_traits>

#define GAS __attribute__((address_space(1)))
#define LAS __attribute__((address_space(3)))
typedef unsigned v4u __attribute__((ext_vector_type(4)));
typedef float f32x4 __attribute__((ext_vector_type(4)));
typedef float f32x2 __attribute__((ext_vector_type(2)));
typedef __bf16 bf16x2 __attribute__((ext_vector_type(2)));
typedef __bf16 bf16x8 __attribute__((ext_vector_type(8)));
typedef unsigned short u16;
typedef GAS unsigned gu32;
typedef __amdgpu_buffer_rsrc_t rsrc_t;
#define RLX_AGENT __ATOMIC_RELAXED, __HIP_MEMORY_SCOPE_AGENT

constexpr int D = 640, H = 32, DH = 128, F = 1728;
constexpr float EPS = 1e-5f;
constexpr float SCALE = 0.08838834764831845f, LOG2E = 1.4426950408889634f;
constexpr unsigned SPIN_CAP = 1u << 20;
constexpr int NS = 15, NB = 23;
constexpr int KO_W = 12, KG_W = 1, NEED = 2, PILOT_J = 20;
constexpr int WS_EPOCH = 0;
constexpr int WS_PROG = 1024;
constexpr int WS_ST = 4096;
constexpr int WS_Q = 16384;
constexpr int WS_P1 = WS_Q + 131072;
constexpr int WS_P2 = WS_P1 + 196608;
constexpr int P2_REP = 40960;
constexpr int WS_P3 = WS_P2 + 5 * P2_REP;
constexpr int WS_END = WS_P3 + 27 * 10240;
constexpr int L_QS = 0, L_ML = 64, L_QF = 96, L_TAG = 97, L_PART = 128;
constexpr int RW = 132;
constexpr int L_AB = 0, L_RED = 1024 + 64, L_ST = L_RED + 32 * RW, L_END = L_ST + 64;

__device__ __forceinline__ float dot2(unsigned k, unsigned q, float acc) {
  return __builtin_amdgcn_fdot2_f32_bf16(__builtin_bit_cast(bf16x2, k), __builtin_bit_cast(bf16x2, q), acc, false);
}
template <int CTRL> __device__ __forceinline__ float dpp(float x) {
  return __builtin_bit_cast(float, __builtin_amdgcn_mov_dpp(__builtin_bit_cast(int, x), CTRL, 0xf, 0xf, true));
}
__device__ __forceinline__ float bflo(unsigned u) { return __builtin_bit_cast(float, u << 16); }
__device__ __forceinline__ float bfhi(unsigned u) { return __builtin_bit_cast(float, u & 0xffff0000u); }
__device__ __forceinline__ float asf(unsigned u) { return __builtin_bit_cast(float, u); }
__device__ __forceinline__ unsigned asu(float f) { return __builtin_bit_cast(unsigned, f); }
__device__ __forceinline__ unsigned cvtpk(float lo, float hi) {
  bf16x2 r; r[0] = (__bf16)lo; r[1] = (__bf16)hi; return __builtin_bit_cast(unsigned, r);
}
__device__ __forceinline__ float rbf(float x) { return (float)(__bf16)x; }
constexpr int XOR1 = 0xB1, XOR2 = 0x4E, XOR7 = 0x141, XOR8 = 0x128;
__device__ __forceinline__ float sw16_add(float a, float b) {
  auto s = __builtin_amdgcn_permlane16_swap(asu(a), asu(b), false, false);
  return asf(s[0]) + asf(s[1]);
}
__device__ __forceinline__ float sw32_add(float a, float b) {
  auto s = __builtin_amdgcn_permlane32_swap(asu(a), asu(b), false, false);
  return asf(s[0]) + asf(s[1]);
}
__device__ __forceinline__ float xrow16_sum(float x) { x = sw16_add(x, x); return sw32_add(x, x); }
__device__ __forceinline__ float xrow16_max(float x) {
  auto s = __builtin_amdgcn_permlane16_swap(asu(x), asu(x), false, false);
  x = fmaxf(asf(s[0]), asf(s[1]));
  auto t = __builtin_amdgcn_permlane32_swap(asu(x), asu(x), false, false);
  return fmaxf(asf(t[0]), asf(t[1]));
}
__device__ __forceinline__ float row16_sum(float x) { x += dpp<XOR1>(x); x += dpp<XOR2>(x); x += dpp<XOR7>(x); x += dpp<XOR8>(x); return x; }
__device__ __forceinline__ float row16_max(float x) {
  x = fmaxf(x, dpp<XOR1>(x)); x = fmaxf(x, dpp<XOR2>(x)); x = fmaxf(x, dpp<XOR7>(x)); x = fmaxf(x, dpp<XOR8>(x)); return x;
}
__device__ __forceinline__ float half_sum(float x) { x = row16_sum(x); return sw16_add(x, x); }
__device__ __forceinline__ float wave_sum(float x) { return xrow16_sum(row16_sum(x)); }
__device__ __forceinline__ float wave_max(float x) { return xrow16_max(row16_max(x)); }
template <int CTRL> __device__ __forceinline__ float trm(float lo, float hi, bool sel) {
  const float t1 = lo + dpp<CTRL>(lo), t2 = hi + dpp<CTRL>(hi); return sel ? t2 : t1;
}
constexpr int brev4(int i) { return ((i & 1) << 3) | ((i & 2) << 1) | ((i & 4) >> 1) | ((i & 8) >> 3); }

template <int I, int N> struct Unroll {
  template <typename Fn> __device__ static __forceinline__ void run(Fn&& f) { f(std::integral_constant<int, I>{}); Unroll<I + 1, N>::run(f); }
};
template <int N> struct Unroll<N, N> { template <typename Fn> __device__ static __forceinline__ void run(Fn&&) {} };

#define BAR() asm volatile("s_waitcnt lgkmcnt(0)\n\ts_barrier" ::: "memory")
#define LDSW() asm volatile("s_waitcnt lgkmcnt(0)" ::: "memory")
__device__ __forceinline__ void putp(rsrc_t rs, int off, unsigned tag, float v0, float v1) {
  __builtin_amdgcn_raw_buffer_store_b128(v4u{asu(v0), tag, asu(v1), tag}, rs, off, 0, 16);
}
__device__ __forceinline__ v4u getp(rsrc_t rs, int off) { return __builtin_amdgcn_raw_buffer_load_b128(rs, off, 0, 16); }
template <int N> __device__ __forceinline__ f32x2 gather(rsrc_t rs, int off, int st, unsigned tag) {
  f32x2 s;
  for (unsigned spins = 0;; ++spins) {
    asm volatile("" ::: "memory");
    v4u v[N];
#pragma unroll
    for (int i = 0; i < N; ++i) v[i] = getp(rs, off + i * st);
    unsigned bad = 0u;
    s = f32x2{asf(v[0][0]), asf(v[0][2])};
#pragma unroll
    for (int i = 0; i < N; ++i) { bad |= (v[i][1] ^ tag) | (v[i][3] ^ tag); if (i) s += f32x2{asf(v[i][0]), asf(v[i][2])}; }
    if (__builtin_amdgcn_ballot_w64(bad != 0u) == 0ull || spins > SPIN_CAP) break;
  }
  return s;
}
template <int N> __device__ __forceinline__ f32x2 gather_tr(rsrc_t rs, int off, int st, unsigned tag) {
  v4u v[N];
  unsigned pend = (1u << N) - 1u;
  for (unsigned spins = 0;; ++spins) {
    asm volatile("" ::: "memory");
#pragma unroll
    for (int i = 0; i < N; ++i) if (pend >> i & 1u) v[i] = getp(rs, off + i * st);
#pragma unroll
    for (int i = 0; i < N; ++i) if (__builtin_amdgcn_ballot_w64(((v[i][1] ^ tag) | (v[i][3] ^ tag)) != 0u) == 0ull) pend &= ~(1u << i);
    if (pend == 0u || spins > SPIN_CAP) break;
  }
  f32x2 s = {asf(v[0][0]), asf(v[0][2])};
#pragma unroll
  for (int i = 1; i < N; ++i) s += f32x2{asf(v[i][0]), asf(v[i][2])};
  return s;
}
__device__ __forceinline__ void put_red(float* ldf, int w, int lane, f32x4 y) {
  *(f32x2*)(ldf + L_RED + w * RW + 2 * lane) = f32x2{y[0], y[1]};
  *(f32x2*)(ldf + L_RED + (16 + w) * RW + 2 * lane) = f32x2{y[2], y[3]};
}
__device__ __forceinline__ void issue_w(v4u (&r)[2][8], const GAS u16* p, int stride) {
#pragma unroll
  for (int ks = 0; ks < 2; ++ks)
#pragma unroll
    for (int jj = 0; jj < 8; ++jj) r[ks][jj] = __builtin_nontemporal_load((const GAS v4u*)(p + (size_t)(32 * ks + jj) * stride));
  __builtin_amdgcn_sched_barrier(0);
}
__device__ __forceinline__ void finish_w(v4u (&bf)[2][8], const v4u (&r)[2][8]) {
#pragma unroll
  for (int ks = 0; ks < 2; ++ks) {
#pragma unroll
    for (int n = 0; n < 8; ++n)
#pragma unroll
      for (int pp = 0; pp < 4; ++pp) bf[ks][n][pp] = __builtin_amdgcn_perm(r[ks][2 * pp + 1][n >> 1], r[ks][2 * pp][n >> 1], (n & 1) ? 0x07060302u : 0x05040100u);
    asm volatile("" : "+v"(bf[ks][0]), "+v"(bf[ks][1]), "+v"(bf[ks][2]), "+v"(bf[ks][3]), "+v"(bf[ks][4]), "+v"(bf[ks][5]), "+v"(bf[ks][6]), "+v"(bf[ks][7]));
    __builtin_amdgcn_sched_barrier(0);
  }
}
__device__ __forceinline__ void load_w(v4u (&bf)[2][8], const GAS u16* p, int stride) { v4u r[2][8]; issue_w(r, p, stride); finish_w(bf, r); }
__device__ __forceinline__ f32x4 mm64(const v4u (&bf)[2][8], const unsigned* ab, int lane) {
  const int g = lane >> 4;
  const v4u a0 = *(const v4u*)(ab + (lane & 1) * 32 + 4 * g), a1 = *(const v4u*)(ab + (lane & 1) * 32 + 16 + 4 * g);
  f32x4 acc[8];
#pragma unroll
  for (int n = 0; n < 8; ++n) acc[n] = __builtin_amdgcn_mfma_f32_16x16x32_bf16(__builtin_bit_cast(bf16x8, a0), __builtin_bit_cast(bf16x8, bf[0][n]), f32x4{0.f, 0.f, 0.f, 0.f}, 0, 0, 0);
#pragma unroll
  for (int n = 0; n < 8; ++n) acc[n] = __builtin_amdgcn_mfma_f32_16x16x32_bf16(__builtin_bit_cast(bf16x8, a1), __builtin_bit_cast(bf16x8, bf[1][n]), acc[n], 0, 0, 0);
  const bool g1 = g & 1, g2 = g & 2;
  f32x4 o;
#pragma unroll
  for (int e = 0; e < 2; ++e)
#pragma unroll
    for (int bb = 0; bb < 2; ++bb) {
      const float lo = g1 ? acc[2 + e][bb] : acc[e][bb], hi = g1 ? acc[6 + e][bb] : acc[4 + e][bb];
      o[2 * bb + e] = g2 ? hi : lo;
    }
  return o;
}

__global__ void __launch_bounds__(1024, 4) k_fused(const u16* __restrict__ x, const u16* __restrict__ kc, const u16* __restrict__ vc,
                                                   const u16* __restrict__ wq, const u16* __restrict__ wo, const u16* __restrict__ wg,
                                                   const u16* __restrict__ wu, const u16* __restrict__ wd, u16* __restrict__ out,
                                                   unsigned char* __restrict__ wsp) {
  __shared__ __attribute__((aligned(16))) unsigned lds[L_END];
  float* ldf = (float*)lds;
  const int tid = threadIdx.x, lane = tid & 63, w = __builtin_amdgcn_readfirstlane(tid >> 6), g = lane >> 4, c = lane & 15;
  const int bid = blockIdx.x, xcd = bid & 7, j = bid >> 3;
  const rsrc_t rs = __builtin_amdgcn_make_buffer_rsrc((void*)wsp, 0, WS_END, 0x00020000);
  if (j >= 8) {
    const int a = xcd * 24 + (j - 8), bh = a / 3, s = a - bh * 3;
    const int b0 = s * 342 - (s >> 1), nb = s == 0 ? 342 : 341;
    if (w > 0) {
      const int sw = w - 1;
      const size_t kvoff = ((size_t)bh * 1024 + b0) * 512;
      const rsrc_t rk = __builtin_amdgcn_make_buffer_rsrc((void*)(kc + kvoff), 0, nb * 1024, 0x00020000);
      const rsrc_t rv = __builtin_amdgcn_make_buffer_rsrc((void*)(vc + kvoff), 0, nb * 1024, 0x00020000);
      const int voff = lane * 16 + sw * 1024;
      v4u kv[NB];
      __builtin_amdgcn_s_setprio(2);
#pragma unroll
      for (int i = 0; i < NB; ++i) {
        kv[i] = __builtin_amdgcn_raw_buffer_load_b128(rk, voff, i * (NS * 1024), 2);
        __builtin_amdgcn_sched_barrier(0);
        if (i == 1) BAR();
      }
      for (unsigned spins = 0; spins < SPIN_CAP; ++spins) {
        if (__builtin_amdgcn_readfirstlane(*(volatile LAS unsigned*)(lds + L_QF))) break;
        __builtin_amdgcn_s_sleep(4);
      }
      const v4u q = *(volatile LAS v4u*)(lds + L_QS + 4 * c);
      float t0, t1, t2, t3, p[2];
      f32x2 acc[4] = {{0.f, 0.f}, {0.f, 0.f}, {0.f, 0.f}, {0.f, 0.f}};
      Unroll<0, 32>::run([&](auto I) {
        constexpr int i = I.value;
        float d = 0.f;
        if constexpr (i < NB) {
          const v4u cur = kv[i];
          d = dot2(cur[0], q[0], 0.f); d = dot2(cur[1], q[1], d); d = dot2(cur[2], q[2], d); d = dot2(cur[3], q[3], d);
          asm volatile("" : "+v"(d));
        }
        if constexpr ((i & 1) == 0) t0 = d;
        else {
          float y = trm<XOR8>(t0, d, c & 8);
          if constexpr ((i & 2) == 0) t1 = y;
          else {
            y = trm<XOR7>(t1, y, c & 4);
            if constexpr ((i & 4) == 0) t2 = y;
            else {
              y = trm<XOR2>(t2, y, c & 2);
              if constexpr ((i & 8) == 0) t3 = y;
              else p[i >> 4] = rbf(rbf(trm<XOR1>(t3, y, c & 1)) * SCALE);
            }
          }
        }
        __builtin_amdgcn_sched_barrier(0);
      });
      {
        const int idx = ((c & 1) << 3) | ((c & 2) << 1) | ((c & 4) >> 1) | ((c & 8) >> 3);
        if (sw + (16 + idx) * NS >= nb) p[1] = -INFINITY;
        const float m = wave_max(fmaxf(p[0], p[1]));
        p[0] = __builtin_amdgcn_exp2f((p[0] - m) * LOG2E); p[1] = __builtin_amdgcn_exp2f((p[1] - m) * LOG2E);
        const float l = wave_sum(p[0] + p[1]);
        if (lane == 0) *(volatile LAS f32x2*)(ldf + L_ML + 2 * sw) = f32x2{m, l};
      }
      if (j == PILOT_J && w == KO_W && lane < 4) putp(rs, WS_PROG + (4 + lane) * 128 + xcd * 16, lds[L_TAG], 0.f, 0.f);
      __builtin_amdgcn_s_setprio(0);
#pragma unroll
      for (int i = 0; i < NB; ++i) { kv[i] = __builtin_amdgcn_raw_buffer_load_b128(rv, voff, i * (NS * 1024), 2); __builtin_amdgcn_sched_barrier(0); }
      Unroll<0, NB>::run([&](auto I) {
        constexpr int i = I.value;
        const v4u cur = kv[i];
        const float pb = dpp<0x150 + brev4(i & 15)>(p[i >> 4]);
#pragma unroll
        for (int jj = 0; jj < 4; ++jj) acc[jj] = __builtin_elementwise_fma(f32x2{pb, pb}, f32x2{bflo(cur[jj]), bfhi(cur[jj])}, acc[jj]);
        asm volatile("" : "+v"(acc[0]), "+v"(acc[1]), "+v"(acc[2]), "+v"(acc[3]));
        __builtin_amdgcn_sched_barrier(0);
      });
      const float u0 = sw16_add(acc[0][0], acc[2][0]), u1 = sw16_add(acc[0][1], acc[2][1]), u2 = sw16_add(acc[1][0], acc[3][0]), u3 = sw16_add(acc[1][1], acc[3][1]);
      *(f32x2*)(ldf + L_PART + sw * 128 + 8 * c + 4 * (g & 1) + 2 * (g >> 1)) = f32x2{sw32_add(u0, u2), sw32_add(u1, u3)};
      if (j == PILOT_J && w == KG_W && lane < 4) putp(rs, WS_PROG + lane * 128 + xcd * 16, lds[L_TAG], 0.f, 0.f);
      BAR();
      return;
    }
    __builtin_amdgcn_s_setprio(3);
    if (lane < 16) *(f32x2*)(ldf + L_ML + 2 * lane) = f32x2{-INFINITY, lane < NS ? -1.f : 0.f};
    if (lane == 0) lds[L_QF] = 0u;
    BAR();
    const unsigned ep = __hip_atomic_load((gu32*)(wsp + WS_EPOCH), RLX_AGENT);
    const int fu = xcd * 10 + (j - 8);
    const bool isF = j < 18;
    const int fb = isF ? fu / 40 : 0, fp = isF ? (fu - fb * 40) * 8 + (lane & 7) : 0;
    const unsigned fx = ((const GAS unsigned*)x)[fb * 320 + fp];
    const unsigned tch = __builtin_nontemporal_load((const GAS unsigned*)(vc + ((size_t)bh * 1024 + b0 + (lane & 1) * (nb - 1)) * 512));
    const unsigned tag = __builtin_amdgcn_readfirstlane(ep) * 1664525u + 1013904223u;
    {
      const f32x2 qv = gather<2>(rs, WS_Q + ((bh & 31) * 4 + (bh >> 5)) * 1024 + lane * 16, 2048, tag);
      lds[L_QS + lane] = cvtpk(qv[0], qv[1]);
      if (lane == 0) lds[L_TAG] = tag;
      LDSW();
      if (lane == 0) *(volatile LAS unsigned*)(lds + L_QF) = 1u;
      asm volatile("" :: "v"(tch));
    }
    f32x2 ml;
    for (unsigned spins = 0;; ++spins) {
      ml = *(volatile LAS f32x2*)(ldf + L_ML + 2 * c);
      if (__all(!(ml[1] < 0.f)) || spins > SPIN_CAP) break;
      __builtin_amdgcn_s_sleep(8);
    }
    float fw;
    {
      const float M = row16_max(ml[0]);
      const float L = row16_sum(ml[1] * __builtin_amdgcn_exp2f((ml[0] - M) * LOG2E));
      if (lane == 0) putp(rs, WS_ST + bh * 128 + s * 16, tag, M, L);
      const int sl = lane & 3;
      const f32x2 st = gather<1>(rs, WS_ST + bh * 128 + (sl < 3 ? sl : 0) * 16, 0, tag);
      float Mg = fmaxf(st[0], dpp<XOR1>(st[0])); Mg = fmaxf(Mg, dpp<XOR2>(Mg));
      float Lg = sl < 3 ? st[1] * __builtin_amdgcn_exp2f((st[0] - Mg) * LOG2E) : 0.f;
      Lg += dpp<XOR1>(Lg); Lg += dpp<XOR2>(Lg);
      fw = __builtin_amdgcn_exp2f((ml[0] - Mg) * LOG2E) / Lg;
    }
    float fs[NS];
#pragma unroll
    for (int i = 0; i < NS; ++i) fs[i] = asf(__builtin_amdgcn_readlane(asu(fw), i));
    BAR();
    f32x2 o0 = {0.f, 0.f}, o1 = {0.f, 0.f};
#pragma unroll
    for (int i = 0; i < NS; ++i) {
      const f32x2 v = *(const f32x2*)(ldf + L_PART + i * 128 + 2 * lane);
      if (i & 1) o1 = __builtin_elementwise_fma(v, f32x2{fs[i], fs[i]}, o1); else o0 = __builtin_elementwise_fma(v, f32x2{fs[i], fs[i]}, o0);
    }
    o0 += o1;
    putp(rs, WS_P1 + (bh * 3 + s) * 1024 + lane * 16, tag, o0[0], o0[1]);
    if (!isF) return;
    const int pg = lane >> 3;
    f32x2 at = gather<1>(rs, WS_P2 + 4 * P2_REP + ((pg & 3) * 2 + fb) * 5120 + fp * 16, 0, tag);
    at[0] += dpp<XOR8>(at[0]); at[1] += dpp<XOR8>(at[1]);
    at[0] = sw16_add(at[0], at[0]); at[1] = sw16_add(at[1], at[1]);
    const float h0 = rbf(bflo(fx) + rbf(at[0])), h1 = rbf(bfhi(fx) + rbf(at[1]));
    f32x2 sm;
    for (unsigned spins = 0;; ++spins) {
      asm volatile("" ::: "memory");
      v4u v[4];
#pragma unroll
      for (int i = 0; i < 4; ++i) v[i] = getp(rs, WS_P3 + min(pg + 8 * i, 26) * 10240 + fb * 5120 + fp * 16);
      unsigned bad = 0u;
#pragma unroll
      for (int i = 0; i < 4; ++i) bad |= (v[i][1] ^ tag) | (v[i][3] ^ tag);
      sm = (f32x2{asf(v[0][0]), asf(v[0][2])} + f32x2{asf(v[1][0]), asf(v[1][2])}) + f32x2{asf(v[2][0]), asf(v[2][2])};
      if (pg < 3) sm += f32x2{asf(v[3][0]), asf(v[3][2])};
      if (__builtin_amdgcn_ballot_w64(bad != 0u) == 0ull || spins > SPIN_CAP) break;
    }
    float s0 = sm[0], s1 = sm[1];
    s0 += dpp<XOR8>(s0); s1 += dpp<XOR8>(s1);
    s0 = xrow16_sum(s0); s1 = xrow16_sum(s1);
    if (lane < 8) ((GAS unsigned*)out)[fb * 320 + fp] = cvtpk(h0 + rbf(s0), h1 + rbf(s1));
    if (fu == 0 && lane == 0) __hip_atomic_store((gu32*)(wsp + WS_EPOCH), tag, RLX_AGENT);
    return;
  }
  const int u = xcd * 8 + j, hb = lane >> 5, i32 = lane & 31;
  unsigned ep = __hip_atomic_load((gu32*)(wsp + WS_EPOCH), RLX_AGENT);
  v4u bf[2][8];
  unsigned* ab = lds + L_AB + w * 64;
  if (w < 5) {
    const int m0 = 5 * (u & 1) + w;
    unsigned xr[10];
#pragma unroll
    for (int m = 0; m < 10; ++m) xr[m] = ((const GAS unsigned*)x)[hb * 320 + i32 + 32 * m];
    const unsigned xo = ((const GAS unsigned*)x)[hb * 320 + i32 + 32 * m0];
    v4u rw[2][8];
    issue_w(rw, (const GAS u16*)wq + (size_t)(64 * m0 + 8 * g) * (H * DH) + (u >> 1) * DH + 8 * c, H * DH);
    float sum = 0.f;
#pragma unroll
    for (int m = 0; m < 10; ++m) sum += bflo(xr[m]) + bfhi(xr[m]);
    const float mean = half_sum(sum) * (1.f / D);
    float sq = 0.f;
#pragma unroll
    for (int m = 0; m < 10; ++m) { const float a0 = bflo(xr[m]) - mean, a1 = bfhi(xr[m]) - mean; sq = fmaf(a0, a0, fmaf(a1, a1, sq)); }
    const float var = half_sum(sq) * (1.f / D);
    const float mb = rbf(mean), sd = rbf(sqrtf(rbf(rbf(var) + EPS)));
    ab[lane] = cvtpk(rbf(bflo(xo) - mb) / sd, rbf(bfhi(xo) - mb) / sd);
    __builtin_amdgcn_sched_barrier(0);
    finish_w(bf, rw);
    LDSW();
    put_red(ldf, w, lane, mm64(bf, ab, lane));
  }
  asm volatile("" : "+v"(ep));
  const unsigned tag = __builtin_amdgcn_readfirstlane(ep) * 1664525u + 1013904223u;
  BAR();
  if (w < 2) {
    const int rdl = 16 * (lane & 3) + (lane >> 2);
    f32x2 sum = {0.f, 0.f};
#pragma unroll
    for (int i = 0; i < 5; ++i) sum += *(const f32x2*)(ldf + L_RED + (16 * w + i) * RW + 2 * rdl);
    putp(rs, WS_Q + (u * 2 + w) * 1024 + lane * 16, tag, sum[0], sum[1]);
  }
  if (xcd >= 4 ? j >= 5 : xcd + 4 * j >= 27) return;
  if (w == 2) {
    for (unsigned spins = 0; spins < SPIN_CAP; ++spins) {
      asm volatile("" ::: "memory");
      const v4u v = getp(rs, WS_PROG + xcd * 128 + (lane & 7) * 16);
      if (__builtin_popcountll(__builtin_amdgcn_ballot_w64(v[1] == tag) & 0xffull) >= NEED) break;
      __builtin_amdgcn_s_sleep(8);
    }
  }
  BAR();
  if (xcd >= 4) {
    const int kg = xcd - 4, hd = 8 * kg + (w >> 1), k0 = hd * DH + 64 * (w & 1);
    load_w(bf, (const GAS u16*)wo + (size_t)(k0 + 8 * g) * D + 128 * j + 8 * c, D);
    BAR();
    const f32x2 o = gather<3>(rs, WS_P1 + (hb * 32 + hd) * 3072 + (w & 1) * 512 + i32 * 16, 1024, tag);
    ab[lane] = cvtpk(o[0], o[1]);
    LDSW();
    put_red(ldf, w, lane, mm64(bf, ab, lane));
    BAR();
    const int part = lane & 7, pi = (w & 7) * 8 + (lane >> 3), bb = w >> 3;
    const float* rp = ldf + L_RED + (16 * bb + 2 * part) * RW + 2 * (16 * (pi & 3) + (pi >> 2));
    const f32x2 sum = *(const f32x2*)rp + *(const f32x2*)(rp + RW);
    float s0 = sum[0], s1 = sum[1];
    s0 += dpp<XOR1>(s0); s1 += dpp<XOR1>(s1); s0 += dpp<XOR2>(s0); s1 += dpp<XOR2>(s1); s0 += dpp<XOR7>(s0); s1 += dpp<XOR7>(s1);
    if (part < 5) putp(rs, WS_P2 + part * P2_REP + (kg * 2 + bb) * 5120 + (64 * j + pi) * 16, tag, s0, s1);
    return;
  }
  const int gi = xcd + 4 * j;
  if (w == 15) return;
  unsigned xv = 0;
  if (w < 10) {
    load_w(bf, (const GAS u16*)(c < 8 ? wg : wu) + (size_t)(64 * w + 8 * g) * F + 64 * gi + 8 * (c & 7), F);
    xv = ((const GAS unsigned*)x)[hb * 320 + 32 * w + i32];
    ab[lane] = 0x3f803f80u;
    LDSW();
    put_red(ldf, w, lane, mm64(bf, ab, lane));
  } else {
    load_w(bf, (const GAS u16*)wd + (size_t)(64 * gi + 8 * g) * D + 128 * (w - 10) + 8 * c, D);
    if (w < 12) put_red(ldf, w, lane, f32x4{0.f, 0.f, 0.f, 0.f});
  }
  BAR();
  const int fpi = hb * 32 + (w & 3) * 8 + ((lane >> 2) & 7);
  const float* frp = ldf + L_RED + (16 * ((w >> 2) & 1) + (lane & 3)) * RW + 2 * (16 * (fpi & 3) + (fpi >> 2));
  float cs0, cs1;
  {
    const f32x2 t = (*(const f32x2*)frp + *(const f32x2*)(frp + 4 * RW)) + *(const f32x2*)(frp + 8 * RW);
    cs0 = t[0]; cs1 = t[1];
    cs0 += dpp<XOR1>(cs0); cs1 += dpp<XOR1>(cs1); cs0 += dpp<XOR2>(cs0); cs1 += dpp<XOR2>(cs1);
  }
  BAR();
  if (w < 10) {
    const f32x2 at = gather_tr<4>(rs, WS_P2 + xcd * P2_REP + hb * 5120 + (32 * w + i32) * 16, 10240, tag);
    const unsigned hp = cvtpk(bflo(xv) + rbf(at[0]), bfhi(xv) + rbf(at[1]));
    ab[lane] = hp;
    LDSW();
    const f32x4 y = mm64(bf, ab, lane);
    const float h0 = bflo(hp), h1 = bfhi(hp);
    const float mw = half_sum(h0 + h1) * (1.f / 64.f);
    const float m2 = half_sum((h0 - mw) * (h0 - mw) + (h1 - mw) * (h1 - mw));
    put_red(ldf, w, lane, y);
    if (i32 == 0) *(f32x2*)(ldf + L_ST + (hb * 10 + w) * 2) = f32x2{mw, m2};
  }
  BAR();
  float r0 = 0.f, r1 = 0.f;
  if (w < 8) {
    const float sm = c < 10 ? ldf[L_ST + ((w >> 2) * 10 + c) * 2] : 0.f;
    const f32x2 t = (*(const f32x2*)frp + *(const f32x2*)(frp + 4 * RW)) + *(const f32x2*)(frp + 8 * RW);
    const float mb = rbf(row16_sum(sm) * 0.1f);
    float a0 = t[0], a1 = t[1];
    a0 += dpp<XOR1>(a0); a1 += dpp<XOR1>(a1); a0 += dpp<XOR2>(a0); a1 += dpp<XOR2>(a1);
    const unsigned vp = cvtpk(fmaf(-mb, cs0, a0), fmaf(-mb, cs1, a1));
    auto sw = __builtin_amdgcn_permlane32_swap(vp, vp, false, false);
    if ((lane & 0x23) == 0) lds[L_AB + 16 * 64 + (w >> 2) * 32 + fpi] = cvtpk(bflo(sw[0]) * bflo(sw[1]), bfhi(sw[0]) * bfhi(sw[1]));
  } else if (w >= 10) {
    const f32x2 st = c < 10 ? *(const f32x2*)(ldf + L_ST + ((g & 1) * 10 + c) * 2) : f32x2{0.f, 0.f};
    const float mean = row16_sum(st[0]) * 0.1f;
    const float dm = c < 10 ? st[0] - mean : 0.f;
    const float var = row16_sum(fmaf(64.f * dm, dm, st[1])) * (1.f / D);
    const float sd = rbf(__builtin_amdgcn_sqrtf(rbf(rbf(var) + EPS)));
    const float rr = __builtin_amdgcn_rcpf(sd * sd);
    auto sw = __builtin_amdgcn_permlane16_swap(asu(rr), asu(rr), false, false);
    r0 = asf(sw[0]); r1 = asf(sw[1]);
  }
  BAR();
  if (w >= 10) {
    const f32x4 y = mm64(bf, lds + L_AB + 16 * 64, lane);
    const int off = WS_P3 + gi * 10240 + (64 * (w - 10) + 4 * c + g) * 16;
    putp(rs, off, tag, y[0] * r0, y[1] * r0);
    putp(rs, off + 5120, tag, y[2] * r1, y[3] * r1);
  }
}

extern "C" void kernel_launch(void* const* d_in, const int* in_sizes, int n_in, void* d_out, int out_size, void* d_ws, size_t ws_size,
                              hipStream_t stream) {
  k_fused<<<256, 1024, 0, stream>>>((const u16*)d_in[0], (const u16*)d_in[1], (const u16*)d_in[2], (const u16*)d_in[3], (const u16*)d_in[4],
                                    (const u16*)d_in[6], (const u16*)d_in[7], (const u16*)d_in[8], (u16*)d_out, (unsigned char*)d_ws);
}
```
